```python
import torch

def _softmax(x, axis=-1):
    return torch.softmax(x, dim=axis)

def _gelu(x):
    return torch.nn.functional.gelu(x, approximate='tanh')

def _silu(x):
    return torch.nn.functional.silu(x)

def _sigmoid(x):
    return torch.sigmoid(x)

def _layernorm(x, axis=-1, eps=1e-05):
    m = x.mean(dim=axis, keepdim=True)
    v = x.var(dim=axis, keepdim=True, unbiased=False)
    return (x - m) / torch.sqrt(v + eps)

def _scatter_add(tgt, idx, src, axis=0):
    return tgt.clone().index_add_(axis, idx, src)

def _segment_sum(src, seg, n):
    out = torch.zeros((n,) + src.shape[1:], dtype=src.dtype)
    return out.index_add_(0, seg, src)

def _pad(x, pw):
    flat = tuple((v for pair in reversed(pw) for v in pair))
    return torch.nn.functional.pad(x, flat)
kernel_name = 'adaLN_cond__sliding_block_cached__glu_gated_bilinear__decode'

def setup_inputs():

    _w = lambda a: (lambda t: t.to(torch.bfloat16) if t.is_floating_point() else t)(torch.as_tensor(a))

    def _A_setup():
        B, Sq, D = (1, 1, 640)
        Hq, Hkv, Sk, Dh = (32, 32, 32768, 192)
        assert Hq % Hkv == 0, (Hq, Hkv)
        return {'x': _w((lambda *s: torch.randn(*s, dtype=torch.bfloat16) * 0.05)(B, Sq, D)), 'k_cache': _w((lambda *s: torch.randn(*s, dtype=torch.bfloat16) * 3.2)(B, Hkv, Sk, Dh)), 'v_cache': _w((lambda *s: torch.randn(*s, dtype=torch.bfloat16) * 3.2)(B, Hkv, Sk, Dh)), 'wq': _w((lambda *s: torch.randn(*s, dtype=torch.bfloat16) * 3.2)(D, Hq * Dh) * D ** (-0.5)), 'wo': _w((lambda *s: torch.randn(*s, dtype=torch.bfloat16) * 3.2)(Hq * Dh, D) * (Hq * Dh) ** (-0.5))}
    _A_in = _A_setup()

    def _B_setup():
        B, S, D, F = (1, 1, 640, 1536)
        return {'x': _w((lambda *s: torch.randn(*s, dtype=torch.bfloat16) * 0.05)(B, S, D)), 'w_gate': _w((lambda *s: torch.randn(*s, dtype=torch.bfloat16) * 3.2)(D, F) * D ** (-0.5)), 'w_up': _w((lambda *s: torch.randn(*s, dtype=torch.bfloat16) * 3.2)(D, F) * D ** (-0.5)), 'w_down': _w((lambda *s: torch.randn(*s, dtype=torch.bfloat16) * 3.2)(F, D) * F ** (-0.5))}
    _B_in = _B_setup()
    return {'A_x': _A_in['x'], 'A_k_cache': _A_in['k_cache'], 'A_v_cache': _A_in['v_cache'], 'A_wq': _A_in['wq'], 'A_wo': _A_in['wo'], 'B_x': _B_in['x'], 'B_w_gate': _B_in['w_gate'], 'B_w_up': _B_in['w_up'], 'B_w_down': _B_in['w_down'], 'c': _w((lambda *s: torch.randn(*s, dtype=torch.bfloat16) * 0.2)(1, 640) * 8.0), 'w_mod': _w((lambda *s: torch.randn(*s, dtype=torch.bfloat16) * 0.2)(640, 6 * 640) * 640 ** (-0.5) * 8.0)}

def reference(A_x, A_k_cache, A_v_cache, A_wq, A_wo, B_x, B_w_gate, B_w_up, B_w_down, c, w_mod):

    def _A_ref(x, k_cache, v_cache, wq, wo):
        B, Sq, D = x.shape
        Hq, Dh = (32, 192)
        Hkv, Sk = (k_cache.shape[1], k_cache.shape[2])
        W = min(4096, max(256, Sk // 4))
        rep = Hq // Hkv
        q = (lambda x, *d: x.permute(*d))((x @ wq).reshape(B, Sq, Hq, Dh), 0, 2, 1, 3)
        k = (lambda x, n, axis: torch.repeat_interleave(x, n, dim=axis))(k_cache[:, :, Sk - W:, :], rep, axis=1)
        v = (lambda x, n, axis: torch.repeat_interleave(x, n, dim=axis))(v_cache[:, :, Sk - W:, :], rep, axis=1)
        scores = torch.einsum('bhqd,bhkd->bhqk', q, k) * Dh ** (-0.5)
        p = _softmax(scores, axis=-1)
        o = torch.einsum('bhqk,bhkd->bhqd', p, v)
        return (lambda x, *d: x.permute(*d))(o, 0, 2, 1, 3).reshape(B, Sq, Hq * Dh) @ wo

    def _B_ref(x, w_gate, w_up, w_down):
        g = torch.einsum('bsd,df->bsf', x, w_gate)
        u = torch.einsum('bsd,df->bsf', x, w_up)
        h = g * u
        return torch.einsum('bsf,fd->bsd', h, w_down)
    x = A_x
    mod = (_silu(c) @ w_mod).reshape(c.shape[0], 6, 1, -1)
    sa, ca, ga, sb, cb, gb = (mod[:, 0].clone(), mod[:, 1].clone(), mod[:, 2].clone(), mod[:, 3].clone(), mod[:, 4].clone(), mod[:, 5].clone())
    h = _layernorm(x, axis=-1) * (1 + ca) + sa
    x = x + ga * _A_ref(h, A_k_cache, A_v_cache, A_wq, A_wo)
    h = _layernorm(x, axis=-1) * (1 + cb) + sb
    x = x + gb * _B_ref(h, B_w_gate, B_w_up, B_w_down)
    return x
```

```cpp
#include <hip/hip_runtime.h>
#include <hip/hip_bf16.h>
#include <stdint.h>

typedef unsigned short u16;
typedef __attribute__((ext_vector_type(4))) unsigned u32x4;

constexpr int D = 640, H = 32, SK = 32768, DH = 192, W = 4096, F = 1536;
constexpr int NSPLIT = 8;

__device__ __forceinline__ float bf2f(u16 x) { return __uint_as_float(((unsigned)x) << 16); }
__device__ __forceinline__ float rbf(float x) {
  return __bfloat162float(__float2bfloat16(x));
}
__device__ __forceinline__ float lo(unsigned u) { return __uint_as_float(u << 16); }
__device__ __forceinline__ float hi(unsigned u) { return __uint_as_float(u & 0xffff0000u); }

template <int MODE>
__global__ void __launch_bounds__(256) gemv_kernel(const float* __restrict__ s_in, const u16* __restrict__ w, int K, int N, float* __restrict__ y) {
  __shared__ float red[32][65];
  int cc = threadIdx.x & 7, kg = threadIdx.x >> 3;
  int n0 = blockIdx.x * 64 + cc * 8;
  float acc[8] = {0, 0, 0, 0, 0, 0, 0, 0};
  for (int k = kg; k < K; k += 32) {
    u32x4 v = *(const u32x4*)(w + (size_t)k * N + n0);
    float s = s_in[k];
    acc[0] += s * lo(v[0]); acc[1] += s * hi(v[0]);
    acc[2] += s * lo(v[1]); acc[3] += s * hi(v[1]);
    acc[4] += s * lo(v[2]); acc[5] += s * hi(v[2]);
    acc[6] += s * lo(v[3]); acc[7] += s * hi(v[3]);
  }
  for (int i = 0; i < 8; i++) red[kg][cc * 8 + i] = acc[i];
  __syncthreads();
  if (threadIdx.x < 64) {
    float t = 0;
    for (int i = 0; i < 32; i++) t += red[i][threadIdx.x];
    y[blockIdx.x * 64 + threadIdx.x] = rbf(t);
  }
}

__global__ void silu_kernel(const u16* c, float* s) {
  int i = threadIdx.x;
  if (i < D) { float x = bf2f(c[i]); s[i] = rbf(x / (1.f + __expf(-x))); }
}

__device__ void ln_mod(const float* xs  , const float* shift, const float* scale, float* h, float* red) {
  int t = threadIdx.x;
  float x = xs[t];
  float v = x;
  for (int o = 32; o; o >>= 1) v += __shfl_xor(v, o);
  if ((t & 63) == 0) red[t >> 6] = v;
  __syncthreads();
  float sum = 0; for (int i = 0; i < 10; i++) sum += red[i];
  float mean = sum / D;
  __syncthreads();
  float d = x - mean; v = d * d;
  for (int o = 32; o; o >>= 1) v += __shfl_xor(v, o);
  if ((t & 63) == 0) red[t >> 6] = v;
  __syncthreads();
  sum = 0; for (int i = 0; i < 10; i++) sum += red[i];
  float var = rbf(sum / D);
  float m = rbf(mean);
  float den = rbf(sqrtf(rbf(var + 1e-5f)));
  float ln = rbf(rbf(x - m) / den);
  h[t] = rbf(rbf(ln * rbf(1.f + scale[t])) + shift[t]);
}

__global__ void __launch_bounds__(640) h1_kernel(const u16* x, const float* mod, float* h) {
  __shared__ float xs[D]; __shared__ float red[16];
  xs[threadIdx.x] = bf2f(x[threadIdx.x]);
  __syncthreads();
  ln_mod(xs, mod, mod + D, h, red);
}

__global__ void __launch_bounds__(640) h2_kernel(const u16* x, const float* mod, const float* attn, float* x1, float* h) {
  __shared__ float xs[D]; __shared__ float red[16];
  int t = threadIdx.x;
  float v = rbf(bf2f(x[t]) + rbf(mod[2 * D + t] * attn[t]));
  xs[t] = v; x1[t] = v;
  __syncthreads();
  ln_mod(xs, mod + 3 * D, mod + 4 * D, h, red);
}

__global__ void gu_kernel(const float* g, const float* u, float* hm) {
  int i = blockIdx.x * 256 + threadIdx.x;
  if (i < F) hm[i] = rbf(g[i] * u[i]);
}

__global__ void out_kernel(const float* x1, const float* mod, const float* y, __hip_bfloat16* out) {
  int t = threadIdx.x;
  out[t] = __float2bfloat16(x1[t] + rbf(mod[5 * D + t] * y[t]));
}

__global__ void __launch_bounds__(256) attn_kernel(const float* q, const u16* kc, const u16* vc, float* po, float* pml) {
  constexpr int KEYS = W / NSPLIT;
  __shared__ float sc[KEYS];
  __shared__ float qs[DH];
  __shared__ float red[8];
  __shared__ float ored[4][DH];
  int h = blockIdx.y, sp = blockIdx.x, t = threadIdx.x;
  if (t < DH) qs[t] = q[h * DH + t];
  __syncthreads();
  const u16* kb = kc + ((size_t)h * SK + (SK - W) + sp * KEYS) * DH;
  const u16* vb = vc + ((size_t)h * SK + (SK - W) + sp * KEYS) * DH;
  int lane = t & 63, wv = t >> 6, r = lane >> 3, c = lane & 7;
  for (int k0 = wv * 8; k0 < KEYS; k0 += 32) {
    float a = 0;
    for (int s = 0; s < 3; s++) {
      u32x4 v = *(const u32x4*)(kb + (size_t)(k0 + r) * DH + s * 64 + c * 8);
      const float* qq = qs + s * 64 + c * 8;
      a += qq[0] * lo(v[0]) + qq[1] * hi(v[0]) + qq[2] * lo(v[1]) + qq[3] * hi(v[1]) + qq[4] * lo(v[2]) + qq[5] * hi(v[2]) + qq[6] * lo(v[3]) + qq[7] * hi(v[3]);
    }
    a += __shfl_xor(a, 1); a += __shfl_xor(a, 2); a += __shfl_xor(a, 4);
    if (c == 0) sc[k0 + r] = rbf(rbf(a) * 0.07216878364870322f);
  }
  __syncthreads();
  float m = -INFINITY;
  for (int i = t; i < KEYS; i += 256) m = fmaxf(m, sc[i]);
  for (int o = 32; o; o >>= 1) m = fmaxf(m, __shfl_xor(m, o));
  if (lane == 0) red[wv] = m;
  __syncthreads();
  m = fmaxf(fmaxf(red[0], red[1]), fmaxf(red[2], red[3]));
  __syncthreads();
  float l = 0;
  for (int i = t; i < KEYS; i += 256) { float p = __expf(sc[i] - m); sc[i] = p; l += p; }
  for (int o = 32; o; o >>= 1) l += __shfl_xor(l, o);
  if (lane == 0) red[4 + wv] = l;
  __syncthreads();
  l = red[4] + red[5] + red[6] + red[7];
  float acc[24];
  for (int i = 0; i < 24; i++) acc[i] = 0;
  for (int k0 = wv * 8; k0 < KEYS; k0 += 32) {
    float p = sc[k0 + r];
    for (int s = 0; s < 3; s++) {
      u32x4 v = *(const u32x4*)(vb + (size_t)(k0 + r) * DH + s * 64 + c * 8);
      for (int j = 0; j < 4; j++) { acc[s * 8 + 2 * j] += p * lo(v[j]); acc[s * 8 + 2 * j + 1] += p * hi(v[j]); }
    }
  }
  for (int i = 0; i < 24; i++) { float a = acc[i]; a += __shfl_xor(a, 8); a += __shfl_xor(a, 16); a += __shfl_xor(a, 32); acc[i] = a; }
  if (r == 0) for (int s = 0; s < 3; s++) for (int j = 0; j < 8; j++) ored[wv][s * 64 + c * 8 + j] = acc[s * 8 + j];
  __syncthreads();
  if (t < DH) po[(h * NSPLIT + sp) * DH + t] = ored[0][t] + ored[1][t] + ored[2][t] + ored[3][t];
  if (t == 0) { pml[(h * NSPLIT + sp) * 2] = m; pml[(h * NSPLIT + sp) * 2 + 1] = l; }
}

__global__ void combine_kernel(const float* po, const float* pml, float* o) {
  int h = blockIdx.x, t = threadIdx.x;
  float M = -INFINITY;
  for (int i = 0; i < NSPLIT; i++) M = fmaxf(M, pml[(h * NSPLIT + i) * 2]);
  float L = 0, a = 0;
  for (int i = 0; i < NSPLIT; i++) { float w = __expf(pml[(h * NSPLIT + i) * 2] - M); L += w * pml[(h * NSPLIT + i) * 2 + 1]; a += w * po[(h * NSPLIT + i) * DH + t]; }
  o[h * DH + t] = rbf(a / L);
}

extern "C" void kernel_launch(void* const* d_in, const int* in_sizes, int n_in, void* d_out, int out_size, void* d_ws, size_t ws_size, hipStream_t stream) {
  const u16* x = (const u16*)d_in[0];
  const u16* kc = (const u16*)d_in[1];
  const u16* vc = (const u16*)d_in[2];
  const u16* wq = (const u16*)d_in[3];
  const u16* wo = (const u16*)d_in[4];
  const u16* wg = (const u16*)d_in[6];
  const u16* wu = (const u16*)d_in[7];
  const u16* wd = (const u16*)d_in[8];
  const u16* c = (const u16*)d_in[9];
  const u16* wm = (const u16*)d_in[10];
  float* ws = (float*)d_ws;
  float* s = ws; float* mod = ws + 1024; float* h = ws + 8192; float* q = ws + 16384; float* po = ws + 32768; float* pml = ws + 131072;
  float* o = ws + 140000; float* attn = ws + 150000; float* x1 = ws + 151000; float* h2 = ws + 152000; float* g = ws + 153000; float* u = ws + 156000; float* hm = ws + 160000; float* y = ws + 165000;
  silu_kernel<<<1, 640, 0, stream>>>(c, s);
  gemv_kernel<0><<<6 * D / 64, 256, 0, stream>>>(s, wm, D, 6 * D, mod);
  h1_kernel<<<1, 640, 0, stream>>>(x, mod, h);
  gemv_kernel<0><<<H * DH / 64, 256, 0, stream>>>(h, wq, D, H * DH, q);
  attn_kernel<<<dim3(NSPLIT, H), 256, 0, stream>>>(q, kc, vc, po, pml);
  combine_kernel<<<H, DH, 0, stream>>>(po, pml, o);
  gemv_kernel<0><<<D / 64, 256, 0, stream>>>(o, wo, H * DH, D, attn);
  h2_kernel<<<1, 640, 0, stream>>>(x, mod, attn, x1, h2);
  gemv_kernel<0><<<F / 64, 256, 0, stream>>>(h2, wg, D, F, g);
  gemv_kernel<0><<<F / 64, 256, 0, stream>>>(h2, wu, D, F, u);
  gu_kernel<<<F / 256, 256, 0, stream>>>(g, u, hm);
  gemv_kernel<0><<<D / 64, 256, 0, stream>>>(hm, wd, F, D, y);
  out_kernel<<<1, 640, 0, stream>>>(x1, mod, y, (__hip_bfloat16*)d_out);
}
```

```cpp
#define WINL 4
#define LATESM
#include <hip/hip_runtime.h>
#include <hip/hip_bf16.h>
#include <stdint.h>

typedef unsigned short u16;
typedef unsigned long long u64;
typedef __attribute__((ext_vector_type(4))) unsigned u32x4;
typedef __attribute__((ext_vector_type(2))) unsigned u32x2;
typedef __attribute__((ext_vector_type(2))) float f32x2;
typedef __attribute__((ext_vector_type(4))) float f32x4;
typedef __attribute__((ext_vector_type(2))) __bf16 bf16x2;
typedef __amdgpu_buffer_rsrc_t rsrc_t;

constexpr int D = 640, H = 32, SK = 32768, DH = 192, W = 4096, F = 1536;
constexpr int NCHAIN = 64, NSPLIT = 6, NWO = 40, NFFN = 24;
constexpr int NG = 13;
#define VPRE 4
#define NUP 2
#define KVAUX 2
#define W2 9
#define NUP2 2
#define ILV
#define SLK 1
#define STAUX 16
#define PLD "sc1"
#define ROT 0
#define SDEL2 0
#define QSENT 0
#define QSL 4
#define SIDEW 0
#define SDEL 0
#define VMID 2
#define VEV 5
#define STAG 8
#define SENT1 5
#define SENT2 3
#define WLD 6
#define CODE_BYTES 29952
constexpr int AUX_NT = 2, AUX_SC1 = 16;

constexpr unsigned WS_EPOCH = 0, WS_MODA = 1024, WS_MODB = WS_MODA + 20480, WS_Q = WS_MODB + 10240,
                   WS_Y1 = WS_Q + 32 * 192 * 48, WS_Y2 = WS_Y1 + 40960, WS_PART = WS_Y2 + 122880, PART_STRIDE = 2048,
                   WS_END = WS_PART + 192 * PART_STRIDE, WS_DBG = 1u << 20;
static_assert(WS_END <= WS_DBG, "");
constexpr unsigned WS_UNUSED = 0;
constexpr unsigned SPIN_CAP = 1u << 16;

__device__ __forceinline__ float lo(unsigned u) { return __uint_as_float(u << 16); }
__device__ __forceinline__ float hi(unsigned u) { return __uint_as_float(u & 0xffff0000u); }
__device__ __forceinline__ unsigned pack2(float a, float b) {
  f32x2 v = {a, b};
  return __builtin_bit_cast(unsigned, __builtin_convertvector(v, bf16x2));
}
__device__ __forceinline__ float rbf(float x) { return lo(pack2(x, x)); }
__device__ __forceinline__ float dot2(unsigned a, unsigned b, float acc) {
  return __builtin_amdgcn_fdot2_f32_bf16(__builtin_bit_cast(bf16x2, a), __builtin_bit_cast(bf16x2, b), acc, false);
}
template <int CTRL> __device__ __forceinline__ float dpp(float x) {
  return __builtin_bit_cast(float, __builtin_amdgcn_mov_dpp(__builtin_bit_cast(int, x), CTRL, 0xf, 0xf, true));
}
template <int CTRL> __device__ __forceinline__ unsigned dppu(unsigned x) {
  return (unsigned)__builtin_amdgcn_mov_dpp((int)x, CTRL, 0xf, 0xf, true);
}
constexpr int XOR1 = 0xB1, XOR2 = 0x4E, XOR7 = 0x141, ROR8 = 0x128, ROR4 = 0x124, BCAST = 0x150;
__device__ __forceinline__ float tr32(float a, float b) {
  auto s = __builtin_amdgcn_permlane32_swap(__float_as_uint(a), __float_as_uint(b), false, false);
  return __uint_as_float(s[0]) + __uint_as_float(s[1]);
}
__device__ __forceinline__ float tr16(float a, float b) {
  auto s = __builtin_amdgcn_permlane16_swap(__float_as_uint(a), __float_as_uint(b), false, false);
  return __uint_as_float(s[0]) + __uint_as_float(s[1]);
}
__device__ __forceinline__ float max32(float a) {
  auto s = __builtin_amdgcn_permlane32_swap(__float_as_uint(a), __float_as_uint(a), false, false);
  return fmaxf(__uint_as_float(s[0]), __uint_as_float(s[1]));
}
__device__ __forceinline__ float max16(float a) {
  auto s = __builtin_amdgcn_permlane16_swap(__float_as_uint(a), __float_as_uint(a), false, false);
  return fmaxf(__uint_as_float(s[0]), __uint_as_float(s[1]));
}
__device__ __forceinline__ float wave_sum(float v) {
  v += dpp<XOR1>(v); v += dpp<XOR2>(v); v += dpp<XOR7>(v); v += dpp<ROR8>(v);
  v = tr16(v, v); v = tr32(v, v);
  return v;
}
__device__ __forceinline__ rsrc_t mkrsrc(const void* p, unsigned bytes) {
  return __builtin_amdgcn_make_buffer_rsrc((void*)p, 0, bytes, 0x00020000);
}
template <int AUX> __device__ __forceinline__ u32x4 ld16(rsrc_t r, int voff, int soff) {
  return __builtin_amdgcn_raw_buffer_load_b128(r, voff, soff, AUX);
}
template <int AUX> __device__ __forceinline__ u32x2 ld8(rsrc_t r, int voff, int soff) {
  return __builtin_amdgcn_raw_buffer_load_b64(r, voff, soff, AUX);
}
__device__ __forceinline__ void st_gran(rsrc_t r, int voff, unsigned val, unsigned tag) {
  u32x2 v = {val, tag};
  __builtin_amdgcn_raw_buffer_store_b64(v, r, voff, 0, STAUX);
}
__device__ __forceinline__ void st_gran2(rsrc_t r, int voff, unsigned v0, unsigned v1, unsigned tag) {
  u32x4 v = {v0, tag, v1, tag};
  __builtin_amdgcn_raw_buffer_store_b128(v, r, voff, 0, STAUX);
}
#define MEMBAR() asm volatile("" ::: "memory")
#define WAITVM(n) asm volatile("s_waitcnt vmcnt(%0)" ::"n"(n) : "memory")
#define PIN(x) asm volatile("" : "+v"(x))
__device__ __forceinline__ void wg_barrier() {
  asm volatile("s_waitcnt lgkmcnt(0)\n\ts_barrier" ::: "memory");
}
__device__ __forceinline__ void fma8(float* acc, u32x4 v, float s) {
#pragma unroll
  for (int j = 0; j < 4; j++) { acc[2 * j] = fmaf(s, lo(v[j]), acc[2 * j]); acc[2 * j + 1] = fmaf(s, hi(v[j]), acc[2 * j + 1]); }
}
__device__ __forceinline__ void pair_rows(u32x4& A, u32x4& B) {
#pragma unroll
  for (int j = 0; j < 4; j++) {
    unsigned p0 = __builtin_amdgcn_perm(B[j], A[j], 0x05040100u), p1 = __builtin_amdgcn_perm(B[j], A[j], 0x07060302u);
    A[j] = p0; B[j] = p1;
  }
  PIN(A); PIN(B);
}
__device__ __forceinline__ void dot8(float* acc, const u32x4& A, const u32x4& B, unsigned hp) {
#pragma unroll
  for (int j = 0; j < 4; j++) { acc[2 * j] = dot2(A[j], hp, acc[2 * j]); acc[2 * j + 1] = dot2(B[j], hp, acc[2 * j + 1]); }
}
#define STAMP(i)
#define STAMP_DECL
#define STAMP_ARG
#define STAMP_PASS
#define STAMP_FLUSH()
#define SPIN_FAIL(n) (++(n) > SPIN_CAP)

__device__ __forceinline__ void ln_stats(const f32x2 (&xv)[5], float& mean_r, float& rden) {
  float ref = __builtin_bit_cast(float, __builtin_amdgcn_readfirstlane(__builtin_bit_cast(int, xv[0][0])));
  f32x2 r2 = {ref, ref};
  f32x2 d0 = xv[0] - r2, d1 = xv[1] - r2, d2 = xv[2] - r2, d3 = xv[3] - r2, d4 = xv[4] - r2;
  f32x2 s1 = ((d0 + d1) + (d2 + d3)) + d4;
  f32x2 s2 = d0 * d0 + d1 * d1, s3 = d2 * d2 + d3 * d3;
  s2 = d4 * d4 + s2; s2 += s3;
  float v = tr32(s1[0] + s1[1], s2[0] + s2[1]);
  v = tr16(v, v);
  v += dpp<XOR1>(v); v += dpp<XOR2>(v); v += dpp<XOR7>(v); v += dpp<ROR8>(v);
  float md = __builtin_bit_cast(float, __builtin_amdgcn_readlane(__builtin_bit_cast(int, v), 0)) * (1.f / D);
  float sq = __builtin_bit_cast(float, __builtin_amdgcn_readlane(__builtin_bit_cast(int, v), 32)) * (1.f / D);
  float var = fmaxf(fmaf(-md, md, sq), 0.f);
  mean_r = rbf(ref + md);
  rden = __builtin_amdgcn_rcpf(rbf(__builtin_amdgcn_sqrtf(rbf(rbf(var) + 1e-5f))));
}
__device__ __forceinline__ f32x2 rbf2(f32x2 v) { unsigned u = pack2(v[0], v[1]); return f32x2{lo(u), hi(u)}; }

__device__ __forceinline__ void sdot8(float* acc, const u32x4& v, unsigned slo, unsigned shi) {
#pragma unroll
  for (int j = 0; j < 4; j++) { acc[2 * j] = dot2(v[j], slo, acc[2 * j]); acc[2 * j + 1] = dot2(v[j], shi, acc[2 * j + 1]); }
}
template <int I> __device__ __forceinline__ unsigned bc8(unsigned v) {
  unsigned t = (unsigned)__builtin_amdgcn_update_dpp(0, (int)v, BCAST + I, 0xf, 0x3, false);
  return (unsigned)__builtin_amdgcn_update_dpp((int)t, (int)v, BCAST + 8 + I, 0xf, 0xc, false);
}
constexpr int DROP = 0x40000000;

#define TOUCH(x) (x)
constexpr int L_TAG = 96, L_QF = 100, L_ML = 101, L_TRIG = 102, L_PROG = 103, L_F = 104, L_M = 128, L_L = 136, L_RED = 256;
constexpr int GSTR = 42;
constexpr int GB = 8 * DH * 2;
__device__ __forceinline__ int lds_addr(const void* p) { return (int)(unsigned long long)(const __attribute__((address_space(3))) void*)p; }
__device__ __forceinline__ unsigned flag_rd(const unsigned* p) {
  unsigned v;
  asm volatile("ds_read_b32 %0, %1\n\ts_waitcnt lgkmcnt(0)" : "=v"(v) : "v"(lds_addr(p)) : "memory");
  return (unsigned)__builtin_amdgcn_readfirstlane(v);
}
__device__ __forceinline__ void flag_wr(unsigned* p, unsigned v) { asm volatile("ds_write_b32 %0, %1" ::"v"(lds_addr(p)), "v"(v) : "memory"); }
#define LDSU(i) flag_rd(ldsu + (i))

__device__ __forceinline__ void lockstep(unsigned* ldsu, int step) {
  asm volatile("ds_add_u32 %0, %1" ::"v"(lds_addr(ldsu + L_PROG)), "v"(1u) : "memory");
  if (step > SLK) { for (unsigned n = 0; LDSU(L_PROG) < 7u * 64u * (step - SLK) && n < SPIN_CAP; n++) __builtin_amdgcn_s_sleep(1); }
}
__device__ __forceinline__ float score8(const u32x4 (&k)[3], const u32x4 (&q)[3]) {
  float a = 0.f, b = 0.f;
#pragma unroll
  for (int s = 0; s < 3; s++) {
    a = dot2(k[s][0], q[s][0], a); b = dot2(k[s][1], q[s][1], b);
    a = dot2(k[s][2], q[s][2], a); b = dot2(k[s][3], q[s][3], b);
  }
  a += b;
  a += dpp<XOR1>(a); a += dpp<XOR2>(a); a += dpp<XOR7>(a);
  return rbf(rbf(a) * 0.07216878364870322f);
}
template <int N>
__device__ __forceinline__ float wave_stats(const float (&sc)[N], unsigned (&pb)[N], rsrc_t rws, int pml_off, float* lds, int wave, int lane) {
  unsigned* ldsu = (unsigned*)lds;
  float m = sc[0];
#pragma unroll
  for (int t = 1; t < N; t++) m = fmaxf(m, sc[t]);
  m = fmaxf(m, dpp<ROR8>(m)); m = max16(m); m = max32(m);
  float l = 0.f;
#pragma unroll
  for (int t = 0; t < N; t++) { float p = __expf(sc[t] - m); l += p; pb[t] = pack2(p, 0.f); PIN(pb[t]); }
  l += dpp<ROR8>(l); l = tr16(l, l); l = tr32(l, l);
  unsigned old = 0;
  if (lane == 0) {
    lds[L_M + wave] = m; lds[L_L + wave] = l;
    asm volatile("s_waitcnt lgkmcnt(0)" ::: "memory");
    old = __hip_atomic_fetch_add(ldsu + L_ML, 1u, __ATOMIC_RELAXED, __HIP_MEMORY_SCOPE_WORKGROUP);
  }
  MEMBAR();
  if (__builtin_amdgcn_readfirstlane(old) == 6u) {
    int l7 = (lane & 7) < 7 ? (lane & 7) : 6;
    float mw = lds[L_M + l7], lw = (lane & 7) < 7 ? lds[L_L + l7] : 0.f, M = mw;
    M = fmaxf(M, dpp<XOR1>(M)); M = fmaxf(M, dpp<XOR2>(M)); M = fmaxf(M, dpp<XOR7>(M));
    float L = __expf(mw - M) * lw;
    L += dpp<XOR1>(L); L += dpp<XOR2>(L); L += dpp<XOR7>(L);
    st_gran2(rws, lane == 0 ? pml_off : DROP, __float_as_uint(M), __float_as_uint(L), ldsu[L_TAG]);
  }
  MEMBAR();
  return m;
}
__device__ __forceinline__ float wave_factor(float m, float* lds) {
  unsigned* ldsu = (unsigned*)lds;
  while (LDSU(L_ML) < 7u) __builtin_amdgcn_s_sleep(1);
  MEMBAR();
  f32x4 m0 = *(const f32x4*)(lds + L_M), m1 = *(const f32x4*)(lds + L_M + 4);
  float M = fmaxf(fmaxf(fmaxf(m0[0], m0[1]), fmaxf(m0[2], m0[3])), fmaxf(fmaxf(m1[0], m1[1]), m1[2]));
  float fw = __expf(m - M);
  PIN(fw);
  return fw;
}
__device__ __forceinline__ void put_partial(const float (&acc)[24], float fw, float* lds, int wave, int lane) {
  int rho = lane >> 4, e = ((rho & 1) << 1) | (rho >> 1);
  float* op = lds + L_RED + wave * DH + (lane & 7) * 8 + ((lane & 8) >> 1) + e;
#pragma unroll
  for (int i = 0; i < 3; i++) {
    float a0 = tr16(tr32(acc[8 * i], acc[8 * i + 1]), tr32(acc[8 * i + 2], acc[8 * i + 3]));
    float a1 = tr16(tr32(acc[8 * i + 4], acc[8 * i + 5]), tr32(acc[8 * i + 6], acc[8 * i + 7]));
    a0 += dpp<ROR8>(a0); a1 += dpp<ROR8>(a1);
    op[i * 64] = ((lane & 8) ? a1 : a0) * fw;
  }
}

__device__ __forceinline__ void stream_wave(rsrc_t rk, rsrc_t rv, rsrc_t rws, int g0, int gx, int rot, int pbase, int sp, float* lds, int wave, int lane,
                                            unsigned char* ws STAMP_ARG) {
#define GOFF(t) ((t) == 0 ? gx : (g0 + ((t) - 1 + rot < 12 ? (t) - 1 + rot : (t) - 13 + rot) * GSTR) * GB)
  u32x4 kb[NG][3], vb[NG][3];
  const int voff = (lane >> 3) * (DH * 2) + (lane & 7) * 16;
  unsigned* ldsu = (unsigned*)lds;
  if (SDEL) __builtin_amdgcn_s_sleep(SDEL);
#pragma unroll
  for (int t = 0; t < NG + VPRE; t++) {
    int so = GOFF(t < NG ? t : t - NG);
#pragma unroll
    for (int s = 0; s < 3; s++) {
      if (t < NG) kb[t][s] = ld16<KVAUX>(rk, voff + s * 128, so);
      else vb[t - NG][s] = ld16<KVAUX>(rv, voff + s * 128, so);
    }
    if (3 * t + 3 > WINL && t + NUP < NG + VPRE) WAITVM(WINL); else MEMBAR();
    if (t + 1 < NG + VPRE) lockstep(ldsu, t + 1);
  }
  STAMP(1);
  while (LDSU(L_QF) == 0u) __builtin_amdgcn_s_sleep(1);
  MEMBAR();
  STAMP(2);
  u32x4 q[3];
#pragma unroll
  for (int s = 0; s < 3; s++) q[s] = *(const u32x4*)(ldsu + s * 32 + (lane & 7) * 4);
  float sc[NG];
#pragma unroll
  for (int t = 0; t < NG; t++) {
    sc[t] = score8(kb[t], q);
    PIN(sc[t]);
    if (t % VEV == VEV - 1 && t / VEV < VMID && VPRE + t / VEV < NG) {
      MEMBAR();
#pragma unroll
      for (int s = 0; s < 3; s++) vb[VPRE + t / VEV][s] = ld16<KVAUX>(rv, voff + s * 128, GOFF(VPRE + t / VEV));
      MEMBAR();
    }
  }
  if (gx == DROP) sc[0] = -INFINITY;
  unsigned pb[NG];
  float m = wave_stats<NG>(sc, pb, rws, pbase + sp * PART_STRIDE + 96 * 16, lds, wave, lane);
  STAMP(3);
  float acc[24];
#pragma unroll
  for (int i = 0; i < 24; i++) acc[i] = 0.f;
  float fw = 0.f;
#pragma unroll
  for (int t = 0; t < NG; t++) {
    if (VPRE + VMID + t < NG) {
      if (VPRE + VMID + t + NUP2 < NG) WAITVM(W2);
      lockstep(ldsu, NG + VPRE + t);
#pragma unroll
      for (int s = 0; s < 3; s++) vb[VPRE + VMID + t][s] = ld16<KVAUX>(rv, voff + s * 128, GOFF(VPRE + VMID + t));
      MEMBAR();
    }
    if (t == NG - 2) fw = wave_factor(m, lds);
    unsigned ph = pb[t] << 16;
#pragma unroll
    for (int s = 0; s < 3; s++) sdot8(acc + 8 * s, vb[t][s], pb[t], ph);
#pragma unroll
    for (int i = 0; i < 24; i++) PIN(acc[i]);
    MEMBAR();
  }
  STAMP(4);
  put_partial(acc, fw, lds, wave, lane);
}

__device__ __forceinline__ void side_wave(rsrc_t rk, rsrc_t rv, rsrc_t rws, const u16* x, const u16* wq, int head, int pbase, int sp,
                                          float* lds, int lane, unsigned char* ws STAMP_ARG) {
  const int wave = 7, r8 = lane >> 3, c8 = lane & 7;
  __builtin_amdgcn_s_setprio(3);
  if (SDEL2) __builtin_amdgcn_s_sleep(SDEL2);
  unsigned* ldsu = (unsigned*)lds;
  unsigned tagv, xa, xb, xu[5];
  const int row0 = 8 * (13 * sp + (sp < 2 ? sp : 2)), ng = sp < 2 ? 14 : 13;
  const int ra = row0 + 8 * c8 + r8, rb = row0 + 8 * (8 + c8 < ng ? 8 + c8 : 8) + r8;
#define SMALL_LOADS { tagv = __builtin_amdgcn_raw_buffer_load_b32(rws, 0, WS_EPOCH, AUX_SC1) + 1u; xa = x[ra]; xb = x[rb]; \
    _Pragma("unroll") for (int i = 0; i < 5; i++) xu[i] = ((const unsigned*)x)[lane * 5 + i]; MEMBAR(); }
  float acc[24];
  {
    u32x4 qw[14][3];
    const rsrc_t rq = mkrsrc(wq, D * H * DH * 2);
    const int qvoff = (r8 * (H * DH) + c8 * 8) * 2, qso = (row0 * (H * DH) + DH * head) * 2;
#pragma unroll
    for (int g = 0; g < 14; g++)
#pragma unroll
      for (int s = 0; s < 3; s++) qw[g][s] = ld16<AUX_NT>(rq, qvoff + s * 128, (g < 13 || sp < 2) ? qso + g * (8 * H * DH * 2) : DROP);
    MEMBAR();
    SMALL_LOADS
    const unsigned tag = __builtin_amdgcn_readfirstlane(tagv);
    ldsu[L_TAG] = tag;
    f32x2 xs[5];
#pragma unroll
    for (int i = 0; i < 5; i++) xs[i] = f32x2{lo(xu[i]), hi(xu[i])};
    float mean1, rden1;
    ln_stats(xs, mean1, rden1);
    float lna = rbf(rbf(lo(xa) - mean1) * rden1), lnb = rbf(rbf(lo(xb) - mean1) * rden1);
    PIN(lna); PIN(lnb);
    STAMP(1);
    u32x4 g0, g1, g2, g3;
    for (unsigned n = 0;;) {
      g0 = ld16<AUX_SC1>(rws, ra * 32, WS_MODA); g1 = ld16<AUX_SC1>(rws, ra * 32 + 16, WS_MODA);
      g2 = ld16<AUX_SC1>(rws, rb * 32, WS_MODA); g3 = ld16<AUX_SC1>(rws, rb * 32 + 16, WS_MODA);
      if (__all((g0[1] == tag) & (g0[3] == tag) & (g1[1] == tag) & (g1[3] == tag) & (g2[1] == tag) & (g2[3] == tag) & (g3[1] == tag) & (g3[3] == tag))) break;
      if (SPIN_FAIL(n)) break;
      __builtin_amdgcn_s_sleep(2);
      MEMBAR();
    }
    STAMP(7);
#define HMOD(ln, a, b) pack2(rbf(ln * rbf(1.f + rbf(__uint_as_float(b[0]) + __uint_as_float(b[2])))) + rbf(__uint_as_float(a[0]) + __uint_as_float(a[2])), 0.f)
    unsigned ha = HMOD(lna, g0, g1), hb = HMOD(lnb, g2, g3);
#pragma unroll
    for (int i = 0; i < 24; i++) acc[i] = 0.f;
#define Q_STEP(g, i, hv) { unsigned t = bc8<i>(hv), th = t << 16; sdot8(acc, qw[g][0], t, th); sdot8(acc + 8, qw[g][1], t, th); sdot8(acc + 16, qw[g][2], t, th); }
    Q_STEP(0, 0, ha) Q_STEP(1, 1, ha) Q_STEP(2, 2, ha) Q_STEP(3, 3, ha) Q_STEP(4, 4, ha) Q_STEP(5, 5, ha) Q_STEP(6, 6, ha) Q_STEP(7, 7, ha)
    Q_STEP(8, 0, hb) Q_STEP(9, 1, hb) Q_STEP(10, 2, hb) Q_STEP(11, 3, hb) Q_STEP(12, 4, hb) Q_STEP(13, 5, hb)
    unsigned qp[3];
#pragma unroll
    for (int i = 0; i < 3; i++) {
      float a0 = tr16(tr32(acc[8 * i], acc[8 * i + 1]), tr32(acc[8 * i + 2], acc[8 * i + 3]));
      float a1 = tr16(tr32(acc[8 * i + 4], acc[8 * i + 5]), tr32(acc[8 * i + 6], acc[8 * i + 7]));
      a0 += dpp<ROR8>(a0); a1 += dpp<ROR8>(a1);
      qp[i] = __float_as_uint((lane & 8) ? a1 : a0);
    }
    const int qo = WS_Q + (head * NSPLIT + sp) * 1536;
    st_gran2(rws, qo + lane * 16, qp[0], qp[1], tag);
    st_gran(rws, qo + 1024 + lane * 8, qp[2], tag);
    MEMBAR();
  }
  STAMP(8);
  const unsigned tag = __builtin_amdgcn_readfirstlane(tagv);
  {
    const int qb = WS_Q + head * (NSPLIT * 1536);
    u32x4 qa[NSPLIT];
    u32x2 qc[NSPLIT];
    for (unsigned n = 0;;) {
      bool ok = true;
#pragma unroll
      for (int k = 0; k < NSPLIT; k++) { qa[k] = ld16<AUX_SC1>(rws, lane * 16, qb + k * 1536); qc[k] = ld8<AUX_SC1>(rws, lane * 8 + 1024, qb + k * 1536); }
#pragma unroll
      for (int k = 0; k < NSPLIT; k++) ok &= (qa[k][1] == tag) & (qa[k][3] == tag) & (qc[k][1] == tag);
      if (__all(ok)) break;
      if (SPIN_FAIL(n)) break;
      __builtin_amdgcn_s_sleep(QSL);
      MEMBAR();
    }
#define Q6(v, k) (((__uint_as_float(v[0][k]) + __uint_as_float(v[1][k])) + (__uint_as_float(v[2][k]) + __uint_as_float(v[3][k]))) + (__uint_as_float(v[4][k]) + __uint_as_float(v[5][k])))
    const int rho = lane >> 4, d = c8 * 8 + ((lane & 8) >> 1) + (((rho & 1) << 1) | (rho >> 1));
    unsigned q01 = pack2(Q6(qa, 0), Q6(qa, 2)), q2 = pack2(Q6(qc, 0), 0.f);
    ((u16*)lds)[d] = (u16)q01; ((u16*)lds)[64 + d] = (u16)(q01 >> 16); ((u16*)lds)[128 + d] = (u16)q2;
    asm volatile("s_waitcnt lgkmcnt(0)" ::: "memory");
    flag_wr(ldsu + L_QF, 1u);
  }
  STAMP(2);
  while (LDSU(L_ML) < 7u) __builtin_amdgcn_s_sleep(2);
  STAMP(9);
  {
    const int c16 = lane & 15, l6 = c16 < 6 ? c16 : 5;
    u32x4 g;
    for (unsigned n = 0;;) {
      g = ld16<AUX_SC1>(rws, l6 * PART_STRIDE + 96 * 16, pbase);
      if (__all((g[1] == tag) & (g[3] == tag))) break;
      if (SPIN_FAIL(n)) break;
      __builtin_amdgcn_s_sleep(4);
      MEMBAR();
    }
    float mm = __uint_as_float(g[0]), M = mm;
    M = fmaxf(M, dpp<XOR1>(M)); M = fmaxf(M, dpp<XOR2>(M)); M = fmaxf(M, dpp<XOR7>(M));
    float fe = __expf(mm - M);
    float L = c16 < 6 ? fe * __uint_as_float(g[2]) : 0.f;
    L += dpp<XOR1>(L); L += dpp<XOR2>(L); L += dpp<XOR7>(L);
    if (lane == sp) lds[L_F] = fe / L;
  }
  STAMP(6);
}

__device__ __forceinline__ void poll_part(u32x4 (&r)[12], float& y0, float& y1, rsrc_t rs, int vo, int so, unsigned tag, int skip) {
  unsigned a; int t, c;
  skip = __builtin_amdgcn_readfirstlane(skip);
  asm volatile("" : "+s"(skip));
  asm volatile(
      "s_cmp_lg_u32 %[skip], 0\n\t"
      "s_cbranch_scc1 2f\n\t"
      "s_mov_b32 %[c], 0\n\t"
      "s_mov_b32 %[t], %[so]\n\t"
      "buffer_load_dwordx4 v[208:211], %[vo], %[rs], %[t] offen " PLD "\n\t"
      "s_add_u32 %[t], %[t], 2048\n\t"
      "buffer_load_dwordx4 v[212:215], %[vo], %[rs], %[t] offen " PLD "\n\t"
      "s_add_u32 %[t], %[t], 2048\n\t"
      "buffer_load_dwordx4 v[216:219], %[vo], %[rs], %[t] offen " PLD "\n\t"
      "s_add_u32 %[t], %[t], 2048\n\t"
      "buffer_load_dwordx4 v[220:223], %[vo], %[rs], %[t] offen " PLD "\n\t"
      "s_add_u32 %[t], %[t], 2048\n\t"
      "buffer_load_dwordx4 v[224:227], %[vo], %[rs], %[t] offen " PLD "\n\t"
      "s_add_u32 %[t], %[t], 2048\n\t"
      "buffer_load_dwordx4 v[228:231], %[vo], %[rs], %[t] offen " PLD "\n\t"
      "s_sleep 8\n\t"
      "1:\n\t"
      "s_mov_b32 %[t], %[so]\n\t"
      "buffer_load_dwordx4 v[232:235], %[vo], %[rs], %[t] offen " PLD "\n\t"
      "s_add_u32 %[t], %[t], 2048\n\t"
      "buffer_load_dwordx4 v[236:239], %[vo], %[rs], %[t] offen " PLD "\n\t"
      "s_add_u32 %[t], %[t], 2048\n\t"
      "buffer_load_dwordx4 v[240:243], %[vo], %[rs], %[t] offen " PLD "\n\t"
      "s_add_u32 %[t], %[t], 2048\n\t"
      "buffer_load_dwordx4 v[244:247], %[vo], %[rs], %[t] offen " PLD "\n\t"
      "s_add_u32 %[t], %[t], 2048\n\t"
      "buffer_load_dwordx4 v[248:251], %[vo], %[rs], %[t] offen " PLD "\n\t"
      "s_add_u32 %[t], %[t], 2048\n\t"
      "buffer_load_dwordx4 v[252:255], %[vo], %[rs], %[t] offen " PLD "\n\t"
      "s_waitcnt vmcnt(11)\n\t"
      "v_xor_b32 %[a], %[tag], v209\n\t"
      "v_bitop3_b32 %[a], v211, %[a], %[tag] bitop3:0xde\n\t"
      "s_waitcnt vmcnt(10)\n\t"
      "v_bitop3_b32 %[a], v213, %[a], %[tag] bitop3:0xde\n\t"
      "v_add_f32 %[y0], v208, v212\n\t"
      "v_bitop3_b32 %[a], v215, %[a], %[tag] bitop3:0xde\n\t"
      "v_add_f32 %[y1], v210, v214\n\t"
      "s_waitcnt vmcnt(9)\n\t"
      "v_bitop3_b32 %[a], v217, %[a], %[tag] bitop3:0xde\n\t"
      "v_add_f32 %[y0], %[y0], v216\n\t"
      "v_bitop3_b32 %[a], v219, %[a], %[tag] bitop3:0xde\n\t"
      "v_add_f32 %[y1], %[y1], v218\n\t"
      "s_waitcnt vmcnt(8)\n\t"
      "v_bitop3_b32 %[a], v221, %[a], %[tag] bitop3:0xde\n\t"
      "v_add_f32 %[y0], %[y0], v220\n\t"
      "v_bitop3_b32 %[a], v223, %[a], %[tag] bitop3:0xde\n\t"
      "v_add_f32 %[y1], %[y1], v222\n\t"
      "s_waitcnt vmcnt(7)\n\t"
      "v_bitop3_b32 %[a], v225, %[a], %[tag] bitop3:0xde\n\t"
      "v_add_f32 %[y0], %[y0], v224\n\t"
      "v_bitop3_b32 %[a], v227, %[a], %[tag] bitop3:0xde\n\t"
      "v_add_f32 %[y1], %[y1], v226\n\t"
      "s_waitcnt vmcnt(6)\n\t"
      "v_bitop3_b32 %[a], v229, %[a], %[tag] bitop3:0xde\n\t"
      "v_add_f32 %[y0], %[y0], v228\n\t"
      "v_bitop3_b32 %[a], v231, %[a], %[tag] bitop3:0xde\n\t"
      "v_add_f32 %[y1], %[y1], v230\n\t"
      "v_cmp_ne_u32 vcc, 0, %[a]\n\t"
      "s_cbranch_vccz 2f\n\t"
      "s_mov_b32 %[t], %[so]\n\t"
      "buffer_load_dwordx4 v[208:211], %[vo], %[rs], %[t] offen " PLD "\n\t"
      "s_add_u32 %[t], %[t], 2048\n\t"
      "buffer_load_dwordx4 v[212:215], %[vo], %[rs], %[t] offen " PLD "\n\t"
      "s_add_u32 %[t], %[t], 2048\n\t"
      "buffer_load_dwordx4 v[216:219], %[vo], %[rs], %[t] offen " PLD "\n\t"
      "s_add_u32 %[t], %[t], 2048\n\t"
      "buffer_load_dwordx4 v[220:223], %[vo], %[rs], %[t] offen " PLD "\n\t"
      "s_add_u32 %[t], %[t], 2048\n\t"
      "buffer_load_dwordx4 v[224:227], %[vo], %[rs], %[t] offen " PLD "\n\t"
      "s_add_u32 %[t], %[t], 2048\n\t"
      "buffer_load_dwordx4 v[228:231], %[vo], %[rs], %[t] offen " PLD "\n\t"
      "s_waitcnt vmcnt(11)\n\t"
      "v_xor_b32 %[a], %[tag], v233\n\t"
      "v_bitop3_b32 %[a], v235, %[a], %[tag] bitop3:0xde\n\t"
      "s_waitcnt vmcnt(10)\n\t"
      "v_bitop3_b32 %[a], v237, %[a], %[tag] bitop3:0xde\n\t"
      "v_add_f32 %[y0], v232, v236\n\t"
      "v_bitop3_b32 %[a], v239, %[a], %[tag] bitop3:0xde\n\t"
      "v_add_f32 %[y1], v234, v238\n\t"
      "s_waitcnt vmcnt(9)\n\t"
      "v_bitop3_b32 %[a], v241, %[a], %[tag] bitop3:0xde\n\t"
      "v_add_f32 %[y0], %[y0], v240\n\t"
      "v_bitop3_b32 %[a], v243, %[a], %[tag] bitop3:0xde\n\t"
      "v_add_f32 %[y1], %[y1], v242\n\t"
      "s_waitcnt vmcnt(8)\n\t"
      "v_bitop3_b32 %[a], v245, %[a], %[tag] bitop3:0xde\n\t"
      "v_add_f32 %[y0], %[y0], v244\n\t"
      "v_bitop3_b32 %[a], v247, %[a], %[tag] bitop3:0xde\n\t"
      "v_add_f32 %[y1], %[y1], v246\n\t"
      "s_waitcnt vmcnt(7)\n\t"
      "v_bitop3_b32 %[a], v249, %[a], %[tag] bitop3:0xde\n\t"
      "v_add_f32 %[y0], %[y0], v248\n\t"
      "v_bitop3_b32 %[a], v251, %[a], %[tag] bitop3:0xde\n\t"
      "v_add_f32 %[y1], %[y1], v250\n\t"
      "s_waitcnt vmcnt(6)\n\t"
      "v_bitop3_b32 %[a], v253, %[a], %[tag] bitop3:0xde\n\t"
      "v_add_f32 %[y0], %[y0], v252\n\t"
      "v_bitop3_b32 %[a], v255, %[a], %[tag] bitop3:0xde\n\t"
      "v_add_f32 %[y1], %[y1], v254\n\t"
      "v_cmp_ne_u32 vcc, 0, %[a]\n\t"
      "s_cbranch_vccz 2f\n\t"
      "s_add_u32 %[c], %[c], 1\n\t"
      "s_cmp_lt_u32 %[c], 0x8000\n\t"
      "s_cbranch_scc1 1b\n\t"
      "2:\n\t"
      : "=&{v[208:211]}"(r[0]), "=&{v[212:215]}"(r[1]), "=&{v[216:219]}"(r[2]), "=&{v[220:223]}"(r[3]), "=&{v[224:227]}"(r[4]), "=&{v[228:231]}"(r[5]), "=&{v[232:235]}"(r[6]), "=&{v[236:239]}"(r[7]), "=&{v[240:243]}"(r[8]), "=&{v[244:247]}"(r[9]), "=&{v[248:251]}"(r[10]), "=&{v[252:255]}"(r[11]),
        [y0] "=&v"(y0), [y1] "=&v"(y1), [a] "=&v"(a), [t] "=&s"(t), [c] "=&s"(c)
      : [vo] "v"(vo), [rs] "s"(rs), [so] "s"(so), [tag] "s"(tag), [skip] "s"(skip)
      : "vcc", "scc", "memory");
}
__device__ __forceinline__ void poll_y1(u32x4 (&r)[16], float& y0, float& y1, rsrc_t rs, int vo, int so, unsigned tag, int skip) {
  unsigned a; int t, c;
  skip = __builtin_amdgcn_readfirstlane(skip);
  asm volatile("" : "+s"(skip));
  asm volatile(
      "s_cmp_lg_u32 %[skip], 0\n\t"
      "s_cbranch_scc1 2f\n\t"
      "s_mov_b32 %[c], 0\n\t"
      "s_mov_b32 %[t], %[so]\n\t"
      "buffer_load_dwordx4 v[192:195], %[vo], %[rs], %[t] offen " PLD "\n\t"
      "s_add_u32 %[t], %[t], 5120\n\t"
      "buffer_load_dwordx4 v[196:199], %[vo], %[rs], %[t] offen " PLD "\n\t"
      "s_add_u32 %[t], %[t], 5120\n\t"
      "buffer_load_dwordx4 v[200:203], %[vo], %[rs], %[t] offen " PLD "\n\t"
      "s_add_u32 %[t], %[t], 5120\n\t"
      "buffer_load_dwordx4 v[204:207], %[vo], %[rs], %[t] offen " PLD "\n\t"
      "s_add_u32 %[t], %[t], 5120\n\t"
      "buffer_load_dwordx4 v[208:211], %[vo], %[rs], %[t] offen " PLD "\n\t"
      "s_add_u32 %[t], %[t], 5120\n\t"
      "buffer_load_dwordx4 v[212:215], %[vo], %[rs], %[t] offen " PLD "\n\t"
      "s_add_u32 %[t], %[t], 5120\n\t"
      "buffer_load_dwordx4 v[216:219], %[vo], %[rs], %[t] offen " PLD "\n\t"
      "s_add_u32 %[t], %[t], 5120\n\t"
      "buffer_load_dwordx4 v[220:223], %[vo], %[rs], %[t] offen " PLD "\n\t"
      "s_sleep 8\n\t"
      "1:\n\t"
      "s_mov_b32 %[t], %[so]\n\t"
      "buffer_load_dwordx4 v[224:227], %[vo], %[rs], %[t] offen " PLD "\n\t"
      "s_add_u32 %[t], %[t], 5120\n\t"
      "buffer_load_dwordx4 v[228:231], %[vo], %[rs], %[t] offen " PLD "\n\t"
      "s_add_u32 %[t], %[t], 5120\n\t"
      "buffer_load_dwordx4 v[232:235], %[vo], %[rs], %[t] offen " PLD "\n\t"
      "s_add_u32 %[t], %[t], 5120\n\t"
      "buffer_load_dwordx4 v[236:239], %[vo], %[rs], %[t] offen " PLD "\n\t"
      "s_add_u32 %[t], %[t], 5120\n\t"
      "buffer_load_dwordx4 v[240:243], %[vo], %[rs], %[t] offen " PLD "\n\t"
      "s_add_u32 %[t], %[t], 5120\n\t"
      "buffer_load_dwordx4 v[244:247], %[vo], %[rs], %[t] offen " PLD "\n\t"
      "s_add_u32 %[t], %[t], 5120\n\t"
      "buffer_load_dwordx4 v[248:251], %[vo], %[rs], %[t] offen " PLD "\n\t"
      "s_add_u32 %[t], %[t], 5120\n\t"
      "buffer_load_dwordx4 v[252:255], %[vo], %[rs], %[t] offen " PLD "\n\t"
      "s_waitcnt vmcnt(15)\n\t"
      "v_xor_b32 %[a], %[tag], v193\n\t"
      "v_bitop3_b32 %[a], v195, %[a], %[tag] bitop3:0xde\n\t"
      "s_waitcnt vmcnt(14)\n\t"
      "v_bitop3_b32 %[a], v197, %[a], %[tag] bitop3:0xde\n\t"
      "v_add_f32 %[y0], v192, v196\n\t"
      "v_bitop3_b32 %[a], v199, %[a], %[tag] bitop3:0xde\n\t"
      "v_add_f32 %[y1], v194, v198\n\t"
      "s_waitcnt vmcnt(13)\n\t"
      "v_bitop3_b32 %[a], v201, %[a], %[tag] bitop3:0xde\n\t"
      "v_add_f32 %[y0], %[y0], v200\n\t"
      "v_bitop3_b32 %[a], v203, %[a], %[tag] bitop3:0xde\n\t"
      "v_add_f32 %[y1], %[y1], v202\n\t"
      "s_waitcnt vmcnt(12)\n\t"
      "v_bitop3_b32 %[a], v205, %[a], %[tag] bitop3:0xde\n\t"
      "v_add_f32 %[y0], %[y0], v204\n\t"
      "v_bitop3_b32 %[a], v207, %[a], %[tag] bitop3:0xde\n\t"
      "v_add_f32 %[y1], %[y1], v206\n\t"
      "s_waitcnt vmcnt(11)\n\t"
      "v_bitop3_b32 %[a], v209, %[a], %[tag] bitop3:0xde\n\t"
      "v_add_f32 %[y0], %[y0], v208\n\t"
      "v_bitop3_b32 %[a], v211, %[a], %[tag] bitop3:0xde\n\t"
      "v_add_f32 %[y1], %[y1], v210\n\t"
      "s_waitcnt vmcnt(10)\n\t"
      "v_bitop3_b32 %[a], v213, %[a], %[tag] bitop3:0xde\n\t"
      "v_add_f32 %[y0], %[y0], v212\n\t"
      "v_bitop3_b32 %[a], v215, %[a], %[tag] bitop3:0xde\n\t"
      "v_add_f32 %[y1], %[y1], v214\n\t"
      "s_waitcnt vmcnt(9)\n\t"
      "v_bitop3_b32 %[a], v217, %[a], %[tag] bitop3:0xde\n\t"
      "v_add_f32 %[y0], %[y0], v216\n\t"
      "v_bitop3_b32 %[a], v219, %[a], %[tag] bitop3:0xde\n\t"
      "v_add_f32 %[y1], %[y1], v218\n\t"
      "s_waitcnt vmcnt(8)\n\t"
      "v_bitop3_b32 %[a], v221, %[a], %[tag] bitop3:0xde\n\t"
      "v_add_f32 %[y0], %[y0], v220\n\t"
      "v_bitop3_b32 %[a], v223, %[a], %[tag] bitop3:0xde\n\t"
      "v_add_f32 %[y1], %[y1], v222\n\t"
      "v_cmp_ne_u32 vcc, 0, %[a]\n\t"
      "s_cbranch_vccz 2f\n\t"
      "s_mov_b32 %[t], %[so]\n\t"
      "buffer_load_dwordx4 v[192:195], %[vo], %[rs], %[t] offen " PLD "\n\t"
      "s_add_u32 %[t], %[t], 5120\n\t"
      "buffer_load_dwordx4 v[196:199], %[vo], %[rs], %[t] offen " PLD "\n\t"
      "s_add_u32 %[t], %[t], 5120\n\t"
      "buffer_load_dwordx4 v[200:203], %[vo], %[rs], %[t] offen " PLD "\n\t"
      "s_add_u32 %[t], %[t], 5120\n\t"
      "buffer_load_dwordx4 v[204:207], %[vo], %[rs], %[t] offen " PLD "\n\t"
      "s_add_u32 %[t], %[t], 5120\n\t"
      "buffer_load_dwordx4 v[208:211], %[vo], %[rs], %[t] offen " PLD "\n\t"
      "s_add_u32 %[t], %[t], 5120\n\t"
      "buffer_load_dwordx4 v[212:215], %[vo], %[rs], %[t] offen " PLD "\n\t"
      "s_add_u32 %[t], %[t], 5120\n\t"
      "buffer_load_dwordx4 v[216:219], %[vo], %[rs], %[t] offen " PLD "\n\t"
      "s_add_u32 %[t], %[t], 5120\n\t"
      "buffer_load_dwordx4 v[220:223], %[vo], %[rs], %[t] offen " PLD "\n\t"
      "s_waitcnt vmcnt(15)\n\t"
      "v_xor_b32 %[a], %[tag], v225\n\t"
      "v_bitop3_b32 %[a], v227, %[a], %[tag] bitop3:0xde\n\t"
      "s_waitcnt vmcnt(14)\n\t"
      "v_bitop3_b32 %[a], v229, %[a], %[tag] bitop3:0xde\n\t"
      "v_add_f32 %[y0], v224, v228\n\t"
      "v_bitop3_b32 %[a], v231, %[a], %[tag] bitop3:0xde\n\t"
      "v_add_f32 %[y1], v226, v230\n\t"
      "s_waitcnt vmcnt(13)\n\t"
      "v_bitop3_b32 %[a], v233, %[a], %[tag] bitop3:0xde\n\t"
      "v_add_f32 %[y0], %[y0], v232\n\t"
      "v_bitop3_b32 %[a], v235, %[a], %[tag] bitop3:0xde\n\t"
      "v_add_f32 %[y1], %[y1], v234\n\t"
      "s_waitcnt vmcnt(12)\n\t"
      "v_bitop3_b32 %[a], v237, %[a], %[tag] bitop3:0xde\n\t"
      "v_add_f32 %[y0], %[y0], v236\n\t"
      "v_bitop3_b32 %[a], v239, %[a], %[tag] bitop3:0xde\n\t"
      "v_add_f32 %[y1], %[y1], v238\n\t"
      "s_waitcnt vmcnt(11)\n\t"
      "v_bitop3_b32 %[a], v241, %[a], %[tag] bitop3:0xde\n\t"
      "v_add_f32 %[y0], %[y0], v240\n\t"
      "v_bitop3_b32 %[a], v243, %[a], %[tag] bitop3:0xde\n\t"
      "v_add_f32 %[y1], %[y1], v242\n\t"
      "s_waitcnt vmcnt(10)\n\t"
      "v_bitop3_b32 %[a], v245, %[a], %[tag] bitop3:0xde\n\t"
      "v_add_f32 %[y0], %[y0], v244\n\t"
      "v_bitop3_b32 %[a], v247, %[a], %[tag] bitop3:0xde\n\t"
      "v_add_f32 %[y1], %[y1], v246\n\t"
      "s_waitcnt vmcnt(9)\n\t"
      "v_bitop3_b32 %[a], v249, %[a], %[tag] bitop3:0xde\n\t"
      "v_add_f32 %[y0], %[y0], v248\n\t"
      "v_bitop3_b32 %[a], v251, %[a], %[tag] bitop3:0xde\n\t"
      "v_add_f32 %[y1], %[y1], v250\n\t"
      "s_waitcnt vmcnt(8)\n\t"
      "v_bitop3_b32 %[a], v253, %[a], %[tag] bitop3:0xde\n\t"
      "v_add_f32 %[y0], %[y0], v252\n\t"
      "v_bitop3_b32 %[a], v255, %[a], %[tag] bitop3:0xde\n\t"
      "v_add_f32 %[y1], %[y1], v254\n\t"
      "v_cmp_ne_u32 vcc, 0, %[a]\n\t"
      "s_cbranch_vccz 2f\n\t"
      "s_add_u32 %[c], %[c], 1\n\t"
      "s_cmp_lt_u32 %[c], 0x8000\n\t"
      "s_cbranch_scc1 1b\n\t"
      "2:\n\t"
      : "=&{v[192:195]}"(r[0]), "=&{v[196:199]}"(r[1]), "=&{v[200:203]}"(r[2]), "=&{v[204:207]}"(r[3]), "=&{v[208:211]}"(r[4]), "=&{v[212:215]}"(r[5]), "=&{v[216:219]}"(r[6]), "=&{v[220:223]}"(r[7]), "=&{v[224:227]}"(r[8]), "=&{v[228:231]}"(r[9]), "=&{v[232:235]}"(r[10]), "=&{v[236:239]}"(r[11]), "=&{v[240:243]}"(r[12]), "=&{v[244:247]}"(r[13]), "=&{v[248:251]}"(r[14]), "=&{v[252:255]}"(r[15]),
        [y0] "=&v"(y0), [y1] "=&v"(y1), [a] "=&v"(a), [t] "=&s"(t), [c] "=&s"(c)
      : [vo] "v"(vo), [rs] "s"(rs), [so] "s"(so), [tag] "s"(tag), [skip] "s"(skip)
      : "vcc", "scc", "memory");
}
__device__ __forceinline__ void poll_y2(u32x4 (&r)[9], float& y0, float& y1, rsrc_t rs, int vo, int so, unsigned tag, int skip) {
  unsigned a; int t, c;
  skip = __builtin_amdgcn_readfirstlane(skip);
  asm volatile("" : "+s"(skip));
  asm volatile(
      "s_cmp_lg_u32 %[skip], 0\n\t"
      "s_cbranch_scc1 2f\n\t"
      "s_mov_b32 %[c], 0\n\t"
      "s_mov_b32 %[t], %[so]\n\t"
      "buffer_load_dwordx4 v[220:223], %[vo], %[rs], %[t] offen " PLD "\n\t"
      "s_add_u32 %[t], %[t], 40960\n\t"
      "buffer_load_dwordx4 v[224:227], %[vo], %[rs], %[t] offen " PLD "\n\t"
      "s_add_u32 %[t], %[t], 40960\n\t"
      "buffer_load_dwordx4 v[228:231], %[vo], %[rs], %[t] offen " PLD "\n\t"
      "s_sleep 5\n\t"
      "s_mov_b32 %[t], %[so]\n\t"
      "buffer_load_dwordx4 v[232:235], %[vo], %[rs], %[t] offen " PLD "\n\t"
      "s_add_u32 %[t], %[t], 40960\n\t"
      "buffer_load_dwordx4 v[236:239], %[vo], %[rs], %[t] offen " PLD "\n\t"
      "s_add_u32 %[t], %[t], 40960\n\t"
      "buffer_load_dwordx4 v[240:243], %[vo], %[rs], %[t] offen " PLD "\n\t"
      "s_sleep 5\n\t"
      "1:\n\t"
      "s_mov_b32 %[t], %[so]\n\t"
      "buffer_load_dwordx4 v[244:247], %[vo], %[rs], %[t] offen " PLD "\n\t"
      "s_add_u32 %[t], %[t], 40960\n\t"
      "buffer_load_dwordx4 v[248:251], %[vo], %[rs], %[t] offen " PLD "\n\t"
      "s_add_u32 %[t], %[t], 40960\n\t"
      "buffer_load_dwordx4 v[252:255], %[vo], %[rs], %[t] offen " PLD "\n\t"
      "s_waitcnt vmcnt(8)\n\t"
      "v_xor_b32 %[a], %[tag], v221\n\t"
      "v_bitop3_b32 %[a], v223, %[a], %[tag] bitop3:0xde\n\t"
      "s_waitcnt vmcnt(7)\n\t"
      "v_bitop3_b32 %[a], v225, %[a], %[tag] bitop3:0xde\n\t"
      "v_add_f32 %[y0], v220, v224\n\t"
      "v_bitop3_b32 %[a], v227, %[a], %[tag] bitop3:0xde\n\t"
      "v_add_f32 %[y1], v222, v226\n\t"
      "s_waitcnt vmcnt(6)\n\t"
      "v_bitop3_b32 %[a], v229, %[a], %[tag] bitop3:0xde\n\t"
      "v_add_f32 %[y0], %[y0], v228\n\t"
      "v_bitop3_b32 %[a], v231, %[a], %[tag] bitop3:0xde\n\t"
      "v_add_f32 %[y1], %[y1], v230\n\t"
      "v_cmp_ne_u32 vcc, 0, %[a]\n\t"
      "s_cbranch_vccz 2f\n\t"
      "s_mov_b32 %[t], %[so]\n\t"
      "buffer_load_dwordx4 v[220:223], %[vo], %[rs], %[t] offen " PLD "\n\t"
      "s_add_u32 %[t], %[t], 40960\n\t"
      "buffer_load_dwordx4 v[224:227], %[vo], %[rs], %[t] offen " PLD "\n\t"
      "s_add_u32 %[t], %[t], 40960\n\t"
      "buffer_load_dwordx4 v[228:231], %[vo], %[rs], %[t] offen " PLD "\n\t"
      "s_waitcnt vmcnt(8)\n\t"
      "v_xor_b32 %[a], %[tag], v233\n\t"
      "v_bitop3_b32 %[a], v235, %[a], %[tag] bitop3:0xde\n\t"
      "s_waitcnt vmcnt(7)\n\t"
      "v_bitop3_b32 %[a], v237, %[a], %[tag] bitop3:0xde\n\t"
      "v_add_f32 %[y0], v232, v236\n\t"
      "v_bitop3_b32 %[a], v239, %[a], %[tag] bitop3:0xde\n\t"
      "v_add_f32 %[y1], v234, v238\n\t"
      "s_waitcnt vmcnt(6)\n\t"
      "v_bitop3_b32 %[a], v241, %[a], %[tag] bitop3:0xde\n\t"
      "v_add_f32 %[y0], %[y0], v240\n\t"
      "v_bitop3_b32 %[a], v243, %[a], %[tag] bitop3:0xde\n\t"
      "v_add_f32 %[y1], %[y1], v242\n\t"
      "v_cmp_ne_u32 vcc, 0, %[a]\n\t"
      "s_cbranch_vccz 2f\n\t"
      "s_mov_b32 %[t], %[so]\n\t"
      "buffer_load_dwordx4 v[232:235], %[vo], %[rs], %[t] offen " PLD "\n\t"
      "s_add_u32 %[t], %[t], 40960\n\t"
      "buffer_load_dwordx4 v[236:239], %[vo], %[rs], %[t] offen " PLD "\n\t"
      "s_add_u32 %[t], %[t], 40960\n\t"
      "buffer_load_dwordx4 v[240:243], %[vo], %[rs], %[t] offen " PLD "\n\t"
      "s_waitcnt vmcnt(8)\n\t"
      "v_xor_b32 %[a], %[tag], v245\n\t"
      "v_bitop3_b32 %[a], v247, %[a], %[tag] bitop3:0xde\n\t"
      "s_waitcnt vmcnt(7)\n\t"
      "v_bitop3_b32 %[a], v249, %[a], %[tag] bitop3:0xde\n\t"
      "v_add_f32 %[y0], v244, v248\n\t"
      "v_bitop3_b32 %[a], v251, %[a], %[tag] bitop3:0xde\n\t"
      "v_add_f32 %[y1], v246, v250\n\t"
      "s_waitcnt vmcnt(6)\n\t"
      "v_bitop3_b32 %[a], v253, %[a], %[tag] bitop3:0xde\n\t"
      "v_add_f32 %[y0], %[y0], v252\n\t"
      "v_bitop3_b32 %[a], v255, %[a], %[tag] bitop3:0xde\n\t"
      "v_add_f32 %[y1], %[y1], v254\n\t"
      "v_cmp_ne_u32 vcc, 0, %[a]\n\t"
      "s_cbranch_vccz 2f\n\t"
      "s_add_u32 %[c], %[c], 1\n\t"
      "s_cmp_lt_u32 %[c], 0x8000\n\t"
      "s_cbranch_scc1 1b\n\t"
      "2:\n\t"
      : "=&{v[220:223]}"(r[0]), "=&{v[224:227]}"(r[1]), "=&{v[228:231]}"(r[2]), "=&{v[232:235]}"(r[3]), "=&{v[236:239]}"(r[4]), "=&{v[240:243]}"(r[5]), "=&{v[244:247]}"(r[6]), "=&{v[248:251]}"(r[7]), "=&{v[252:255]}"(r[8]),
        [y0] "=&v"(y0), [y1] "=&v"(y1), [a] "=&v"(a), [t] "=&s"(t), [c] "=&s"(c)
      : [vo] "v"(vo), [rs] "s"(rs), [so] "s"(so), [tag] "s"(tag), [skip] "s"(skip)
      : "vcc", "scc", "memory");
}
__device__ __forceinline__ void keep_regs(u32x4 (&r)[12]) { asm volatile("" :: "{v[208:211]}"(r[0]), "{v[212:215]}"(r[1]), "{v[216:219]}"(r[2]), "{v[220:223]}"(r[3]), "{v[224:227]}"(r[4]), "{v[228:231]}"(r[5]), "{v[232:235]}"(r[6]), "{v[236:239]}"(r[7]), "{v[240:243]}"(r[8]), "{v[244:247]}"(r[9]), "{v[248:251]}"(r[10]), "{v[252:255]}"(r[11])); }
__device__ __forceinline__ void keep_regs(u32x4 (&r)[16]) { asm volatile("" :: "{v[192:195]}"(r[0]), "{v[196:199]}"(r[1]), "{v[200:203]}"(r[2]), "{v[204:207]}"(r[3]), "{v[208:211]}"(r[4]), "{v[212:215]}"(r[5]), "{v[216:219]}"(r[6]), "{v[220:223]}"(r[7]), "{v[224:227]}"(r[8]), "{v[228:231]}"(r[9]), "{v[232:235]}"(r[10]), "{v[236:239]}"(r[11]), "{v[240:243]}"(r[12]), "{v[244:247]}"(r[13]), "{v[248:251]}"(r[14]), "{v[252:255]}"(r[15])); }
__device__ __forceinline__ void keep_regs(u32x4 (&r)[9]) { asm volatile("" :: "{v[220:223]}"(r[0]), "{v[224:227]}"(r[1]), "{v[228:231]}"(r[2]), "{v[232:235]}"(r[3]), "{v[236:239]}"(r[4]), "{v[240:243]}"(r[5]), "{v[244:247]}"(r[6]), "{v[248:251]}"(r[7]), "{v[252:255]}"(r[8])); }

__global__ void __launch_bounds__(512, 2)
fused_kernel(const u16* __restrict__ x, const u16* __restrict__ kc, const u16* __restrict__ vc, const u16* __restrict__ wq,
             const u16* __restrict__ wo, const u16* __restrict__ wg, const u16* __restrict__ wu, const u16* __restrict__ wd,
             const u16* __restrict__ cnd, const u16* __restrict__ wm, u16* __restrict__ out, unsigned char* ws) {
  __shared__ __attribute__((aligned(16))) float lds[4096 + 8 * 4096];
  const int tid = threadIdx.x, lane = tid & 63, wave = __builtin_amdgcn_readfirstlane(tid >> 6);
  const int b = blockIdx.x;
  STAMP_DECL;
  STAMP(0);
  unsigned cpf = 0;
  if (b < NCHAIN && tid < CODE_BYTES / 128) {
    const unsigned* pc = (const unsigned*)(__builtin_amdgcn_s_getpc() & ~(u64)255);
    cpf = *((const __attribute__((address_space(1))) unsigned*)pc + tid * 32);
  }
  asm volatile("" ::"s"(x), "s"(kc), "s"(vc), "s"(wq), "s"(wo), "s"(wg), "s"(wu), "s"(wd), "s"(cnd), "s"(wm), "s"(out), "s"(ws));
  const rsrc_t rws = mkrsrc(ws, WS_END);
  const int rho = lane >> 4, e4 = ((rho & 1) << 1) | (rho >> 1), c16 = lane & 15, r8 = lane >> 3, c8 = lane & 7;
  if (b >= NCHAIN) {
    const int a = b - NCHAIN, head = a / NSPLIT, sp = a % NSPLIT;
    const size_t base = ((size_t)head * SK + (SK - W)) * DH;
    const rsrc_t rk = mkrsrc(kc + base, W * DH * 2), rv = mkrsrc(vc + base, W * DH * 2);
    if (tid < 4) ((unsigned*)lds)[L_QF + tid] = 0u;
    wg_barrier();
    const int gs = sp * 85 + (sp < 2 ? sp : 2), pbase = WS_PART + head * NSPLIT * PART_STRIDE;
    const int sw = SIDEW == 0 ? wave - 1 : wave;
    if (wave != SIDEW) stream_wave(rk, rv, rws, sw * 6 + sp, (sw == 0 || (sw == 1 && sp < 2)) ? (504 + 6 * sw + sp) * GB : DROP, ROT, pbase, sp, lds, sw, lane, ws STAMP_PASS);
    else side_wave(rk, rv, rws, x, wq, head, pbase, sp, lds, lane, ws STAMP_PASS);
    wg_barrier();
    if (tid < 96) {
      f32x2 v[7];
#pragma unroll
      for (int w = 0; w < 7; w++) v[w] = *(const f32x2*)(lds + L_RED + w * DH + 2 * tid);
      unsigned tag = ((unsigned*)lds)[L_TAG];
      float ff = lds[L_F];
      f32x2 o = (((v[0] + v[1]) + (v[2] + v[3])) + ((v[4] + v[5]) + v[6])) * f32x2{ff, ff};
      st_gran2(rws, WS_PART + a * PART_STRIDE + tid * 16, __float_as_uint(o[0]), __float_as_uint(o[1]), tag);
    }
    STAMP(5);
    STAMP_FLUSH();
    return;
  }
  const int j = b;
  const unsigned tagv = __builtin_amdgcn_raw_buffer_load_b32(rws, 0, WS_EPOCH, AUX_SC1) + 1u;
  const rsrc_t rm = mkrsrc(wm, D * 6 * D * 2);
  const int c5 = c8 < 5 ? c8 : 4;
  const int lrow = 40 * wave + 5 * r8 + c5;
  const int mh = j >= 20 ? 1 : 0;
  unsigned cv0 = cnd[lrow], cv1 = cnd[320 + lrow];
  u32x4 mv[5];
  const int mvoff = (5 * r8 * (6 * D) + c8 * 8) * 2;
  {
    int so = j < NWO ? ((320 * mh + 40 * wave) * (6 * D) + 64 * (j - 20 * mh)) * 2 : DROP;
#pragma unroll
    for (int i = 0; i < 5; i++) mv[i] = ld16<AUX_NT>(rm, mvoff, so + i * (6 * D) * 2);
  }
  const int mso = (40 * wave * (6 * D) + 2 * D + 64 * j) * 2;
  MEMBAR();
  asm volatile("" ::"v"(cpf));
  const unsigned tag = __builtin_amdgcn_readfirstlane(tagv);
  unsigned sl0, sl1;
  { float c = lo(cv0); sl0 = pack2(c * __builtin_amdgcn_rcpf(1.f + __expf(-c)), 0.f); c = lo(cv1); sl1 = pack2(c * __builtin_amdgcn_rcpf(1.f + __expf(-c)), 0.f); }
#define MOD_STEP(i, mv, sv) { unsigned t = bc8<i>(sv); sdot8(acc, mv[i], t, t << 16); }
  if (j < NWO) {
    float acc[8] = {0, 0, 0, 0, 0, 0, 0, 0};
    unsigned sv = mh ? sl1 : sl0;
    MOD_STEP(0, mv, sv) MOD_STEP(1, mv, sv) MOD_STEP(2, mv, sv) MOD_STEP(3, mv, sv) MOD_STEP(4, mv, sv)
    float a0 = tr16(tr32(acc[0], acc[1]), tr32(acc[2], acc[3])), a1 = tr16(tr32(acc[4], acc[5]), tr32(acc[6], acc[7]));
    a0 += dpp<ROR8>(a0); a1 += dpp<ROR8>(a1);
    lds[wave * 64 + c8 * 8 + ((lane & 8) >> 1) + e4] = (lane & 8) ? a1 : a0;
    wg_barrier();
    if (tid < 64) {
      float s0 = 0.f;
#pragma unroll
      for (int w = 0; w < 8; w++) s0 += lds[w * 64 + tid];
      int col = 64 * (j - 20 * mh) + tid;
      int isca = col >= D;
      st_gran(rws, WS_MODA + (col - isca * D) * 32 + isca * 16 + mh * 8, __float_as_uint(s0), tag);
    }
  }
  STAMP(1);
  MEMBAR();
  const int f = j - NWO;
  if (j < NWO) {
    const int rg = j / 5, cg = j % 5;
    u32x4 ww[24];
    {
      u32x4 mv1[5], mv2[5];
#pragma unroll
      for (int i = 0; i < 5; i++) mv1[i] = ld16<AUX_NT>(rm, mvoff, mso + i * (6 * D) * 2);
#pragma unroll
      for (int i = 0; i < 5; i++) mv2[i] = ld16<AUX_NT>(rm, mvoff, mso + (320 + i) * (6 * D) * 2);
      const rsrc_t ro = mkrsrc(wo, H * DH * D * 2);
      const int voff = (24 * rho * D + 8 * c16) * 2, so = ((768 * rg + 96 * wave) * D + 128 * cg) * 2;
#pragma unroll
      for (int i = 0; i < 4; i++) ww[i] = ld16<AUX_NT>(ro, voff, so + i * D * 2);
      MEMBAR();
      float acc[8] = {0, 0, 0, 0, 0, 0, 0, 0};
      MOD_STEP(0, mv1, sl0) MOD_STEP(1, mv1, sl0) MOD_STEP(2, mv1, sl0) MOD_STEP(3, mv1, sl0) MOD_STEP(4, mv1, sl0)
      MOD_STEP(0, mv2, sl1) MOD_STEP(1, mv2, sl1) MOD_STEP(2, mv2, sl1) MOD_STEP(3, mv2, sl1) MOD_STEP(4, mv2, sl1)
      float a0 = tr16(tr32(acc[0], acc[1]), tr32(acc[2], acc[3])), a1 = tr16(tr32(acc[4], acc[5]), tr32(acc[6], acc[7]));
      a0 += dpp<ROR8>(a0); a1 += dpp<ROR8>(a1);
      lds[512 + wave * 64 + c8 * 8 + ((lane & 8) >> 1) + e4] = (lane & 8) ? a1 : a0;
      wg_barrier();
      if (tid < 32) {
        float s0 = 0.f, s1 = 0.f;
#pragma unroll
        for (int w = 0; w < 8; w++) { f32x2 v = *(const f32x2*)(lds + 512 + w * 64 + 2 * tid); s0 += v[0]; s1 += v[1]; }
        st_gran(rws, WS_MODB + (j * 32 + tid) * 8, pack2(s0, s1), tag);
      }
      MEMBAR();
#pragma unroll
      for (int i = 4; i < 24; i++) ww[i] = ld16<AUX_NT>(ro, voff, so + i * D * 2);
    }
    STAMP(4);
#pragma unroll
    for (int i = 0; i < 12; i++) pair_rows(ww[2 * i], ww[2 * i + 1]);
    STAMP(5);
    const int head = 4 * rg + (wave >> 1);
    const int pbase = WS_PART + head * NSPLIT * PART_STRIDE;
    const int goff = ((wave & 1) * 48 + 12 * rho + (c16 < 12 ? c16 : 11)) * 16;
    unsigned tc = __builtin_amdgcn_raw_buffer_load_b32(rws, TOUCH(WS_Y1 + (rg * D + cg * 128 + 2 * lane) * 8), 0, AUX_SC1);
    {
      int l6 = lane < 6 ? lane : 5;
      for (unsigned n = 0;;) {
        u32x2 g = ld8<AUX_SC1>(rws, l6 * PART_STRIDE + (wave & 1) * 768, pbase);
        if (__builtin_popcountll(__ballot(g[1] == tag) & 63ull) >= SENT1) break;
        if (SPIN_FAIL(n)) break;
        __builtin_amdgcn_s_sleep(4);
        MEMBAR();
      }
    }
    asm volatile("" ::"v"(tc));
    STAMP(6);
    u32x4 pr[12];
    float o0, o1;
    poll_part(pr, o0, o1, rws, goff, pbase, tag, 0);
    unsigned op = pack2(o0, o1);
    STAMP(7);
    {
      float acc[8] = {0, 0, 0, 0, 0, 0, 0, 0};
#define WO_STEP(i) dot8(acc, ww[2 * i], ww[2 * i + 1], dppu<BCAST + i>(op));
      WO_STEP(0) WO_STEP(1) WO_STEP(2) WO_STEP(3) WO_STEP(4) WO_STEP(5) WO_STEP(6) WO_STEP(7) WO_STEP(8) WO_STEP(9) WO_STEP(10) WO_STEP(11)
      float a0 = tr16(tr32(acc[0], acc[1]), tr32(acc[2], acc[3]));
      float a1 = tr16(tr32(acc[4], acc[5]), tr32(acc[6], acc[7]));
      float* red = lds + 1024;
      red[wave * 128 + c16 * 8 + e4] = a0; red[wave * 128 + c16 * 8 + 4 + e4] = a1;
      wg_barrier();
      if (tid < 64) {
        f32x2 v[8];
#pragma unroll
        for (int w = 0; w < 8; w++) v[w] = *(const f32x2*)(red + w * 128 + 2 * tid);
        f32x2 o = ((v[0] + v[1]) + (v[2] + v[3])) + ((v[4] + v[5]) + (v[6] + v[7]));
        st_gran2(rws, WS_Y1 + (rg * D + cg * 128 + 2 * tid) * 8, __float_as_uint(o[0]), __float_as_uint(o[1]), tag);
      }
    }
    MEMBAR();
    keep_regs(pr);
    STAMP(8);
    STAMP_FLUSH();
    return;
  }
  u32x4 gw[10], uw[10], dw[16];
  {
    const rsrc_t rup = mkrsrc(wu, D * F * 2), rdn = mkrsrc(wd, F * D * 2);
    const rsrc_t rgt = mkrsrc(wg, D * F * 2);
    const int gvoff = (r8 * 10 * F + c8 * 8) * 2, gso = (80 * wave * F + 64 * f) * 2;
#pragma unroll
    for (int i = 0; i < 10; i++) gw[i] = ld16<AUX_NT>(rgt, gvoff, gso + i * F * 2);
#pragma unroll
    for (int i = 0; i < 10; i++) uw[i] = ld16<AUX_NT>(rup, gvoff, gso + i * F * 2);
    int voffd = (16 * rho * D + 8 * c16) * 2;
    int so = wave < 5 ? (64 * f * D + 128 * wave) * 2 : DROP;
#pragma unroll
    for (int i = 0; i < 16; i++) dw[i] = ld16<AUX_NT>(rdn, voffd, so + i * D * 2);
  }
  STAMP(4);
  float* x1s = lds;
  float* sbs = lds + 640;
  float* cbs = lds + 1280;
  float* red = lds + 3072;
  float ga0 = 0.f, ga1 = 0.f;
  unsigned xin = 0;
  const int fin = f * 2 + (wave - 5);
  const bool is_fin = (wave == 5 || wave == 6) && f < 20;
  if (wave < 5) {
    int gi = wave * 64 + lane;
    xin = ((const unsigned*)x)[gi];
    u32x2 g0, g1, g2;
    for (unsigned n = 0;;) {
      g0 = ld8<AUX_SC1>(rws, gi * 8, WS_MODB); g1 = ld8<AUX_SC1>(rws, gi * 8, WS_MODB + 320 * 8); g2 = ld8<AUX_SC1>(rws, gi * 8, WS_MODB + 640 * 8);
      if (__all(g0[1] == tag && g1[1] == tag && g2[1] == tag)) break;
      if (SPIN_FAIL(n)) break;
      __builtin_amdgcn_s_sleep(8);
      MEMBAR();
    }
    ga0 = lo(g0[0]); ga1 = hi(g0[0]);
    *(f32x2*)(sbs + 2 * gi) = f32x2{lo(g1[0]), hi(g1[0])};
    *(f32x2*)(cbs + 2 * gi) = f32x2{rbf(1.f + lo(g2[0])), rbf(1.f + hi(g2[0]))};
  } else if (is_fin) {
    u32x2 g0;
    for (unsigned n = 0;;) {
      g0 = ld8<AUX_SC1>(rws, (fin * 8 + c8) * 8, WS_MODB + 960 * 8);
      if (__all(g0[1] == tag)) break;
      if (SPIN_FAIL(n)) break;
      __builtin_amdgcn_s_sleep(8);
      MEMBAR();
    }
    ga0 = lo(g0[0]); ga1 = hi(g0[0]);
  }
#pragma unroll
  for (int i = 0; i < 5; i++) { pair_rows(gw[2 * i], gw[2 * i + 1]); pair_rows(uw[2 * i], uw[2 * i + 1]); }
#pragma unroll
  for (int i = 0; i < 8; i++) pair_rows(dw[2 * i], dw[2 * i + 1]);
  u32x4* dwl = (u32x4*)(lds + 4096) + wave * 1024 + lane;
#pragma unroll
  for (int i = 0; i < 16; i++) dwl[i * 64] = dw[i];
  wg_barrier();
  const int hrow = 80 * wave + 20 * rho + 2 * (c16 < 10 ? c16 : 9);
  f32x2 sbv = *(const f32x2*)(sbs + hrow), cbv = *(const f32x2*)(cbs + hrow);
  PIN(sbv); PIN(cbv);
  STAMP(5);
  const rsrc_t rout = mkrsrc(out, D * 2);
  {
    unsigned tc = __builtin_amdgcn_raw_buffer_load_b32(rws, TOUCH(wave < 5 ? WS_Y2 + (f * D + wave * 128 + lane * 2) * 8 : WS_Y2 + (lane < 24 ? lane : 0) * D * 8), 0, AUX_SC1);
    tc |= __builtin_amdgcn_raw_buffer_load_b32(rout, TOUCH(0), 0, AUX_SC1);
    asm volatile("" ::"v"(tc));
    if (wave < 5) {
      for (unsigned n = 0;;) {
        u32x2 g = ld8<AUX_SC1>(rws, c8 * (D * 8) + wave * 1024, WS_Y1);
        if (__builtin_popcountll(__ballot(g[1] == tag) & 255ull) >= SENT2) break;
        if (SPIN_FAIL(n)) break;
        __builtin_amdgcn_s_sleep(4);
        MEMBAR();
      }
      STAMP(6);
    }
    u32x4 pr[16], dv[16];
    float y0, y1;
    poll_y1(pr, y0, y1, rws, (wave * 128 + 2 * lane) * 8, WS_Y1, tag, wave >= 5 ? 1 : 0);
    STAMP(7);
    if (wave < 5) {
      f32x2 xn = rbf2(f32x2{lo(xin), hi(xin)} + rbf2(f32x2{ga0, ga1} * rbf2(f32x2{y0, y1})));
      *(f32x2*)(x1s + wave * 128 + 2 * lane) = xn;
    }
    wg_barrier();
    {
      f32x2 xv[5];
#pragma unroll
      for (int i = 0; i < 5; i++) xv[i] = *(const f32x2*)(x1s + i * 128 + 2 * lane);
      f32x2 xh = *(const f32x2*)(x1s + hrow);
      float mean2, rden2;
      ln_stats(xv, mean2, rden2);
      f32x2 hh = rbf2(rbf2(rbf2(xh - f32x2{mean2, mean2}) * f32x2{rden2, rden2}) * cbv) + sbv;
      unsigned hp = pack2(hh[0], hh[1]);
      PIN(hp);
      WAITVM(0);
      keep_regs(pr);
      MEMBAR();
      if (wave < 5) {
#pragma unroll
        for (int i = 0; i < 16; i++) dv[i] = dwl[i * 64];
      }
      MEMBAR();
      float ag[8] = {0, 0, 0, 0, 0, 0, 0, 0}, au[8] = {0, 0, 0, 0, 0, 0, 0, 0};
#define GU_STEP(i) { unsigned t = (unsigned)__builtin_amdgcn_update_dpp(0, (int)hp, BCAST + i, 0xf, 0x3, false); \
                     t = (unsigned)__builtin_amdgcn_update_dpp((int)t, (int)hp, BCAST + 5 + i, 0xf, 0xc, false); \
                     dot8(ag, gw[2 * i], gw[2 * i + 1], t); dot8(au, uw[2 * i], uw[2 * i + 1], t); }
      GU_STEP(0) GU_STEP(1) GU_STEP(2) GU_STEP(3) GU_STEP(4)
      float g0 = tr16(tr32(ag[0], ag[1]), tr32(ag[2], ag[3])), g1 = tr16(tr32(ag[4], ag[5]), tr32(ag[6], ag[7]));
      float u0 = tr16(tr32(au[0], au[1]), tr32(au[2], au[3])), u1 = tr16(tr32(au[4], au[5]), tr32(au[6], au[7]));
      g0 += dpp<ROR8>(g0); g1 += dpp<ROR8>(g1); u0 += dpp<ROR8>(u0); u1 += dpp<ROR8>(u1);
      bool up = lane & 8;
      float* rp = red + wave * 128 + (up ? 64 : 0) + c8 * 8 + e4;
      rp[0] = up ? u0 : g0; rp[4] = up ? u1 : g1;
    }
    f32x2 xo = *(const f32x2*)(x1s + (is_fin ? fin : 0) * 16 + c8 * 2);
    wg_barrier();
    STAMP(8);
    if (wave < 5) {
      f32x2 v[8];
#pragma unroll
      for (int w = 0; w < 8; w++) v[w] = f32x2{red[w * 128 + lane], red[w * 128 + 64 + lane]};
      f32x2 gu = rbf2(((v[0] + v[1]) + (v[2] + v[3])) + ((v[4] + v[5]) + (v[6] + v[7])));
      float hm = gu[0] * gu[1];
      float hn = dpp<XOR1>(hm);
      unsigned hp = pack2(hm, hn);
      float acc[8] = {0, 0, 0, 0, 0, 0, 0, 0};
#define DN_STEP(i) dot8(acc, dv[2 * i], dv[2 * i + 1], dppu<BCAST + 2 * i>(hp));
      DN_STEP(0) DN_STEP(1) DN_STEP(2) DN_STEP(3) DN_STEP(4) DN_STEP(5) DN_STEP(6) DN_STEP(7)
      float a0 = tr16(tr32(acc[0], acc[2]), tr32(acc[4], acc[6]));
      float a1 = tr16(tr32(acc[1], acc[3]), tr32(acc[5], acc[7]));
      st_gran2(rws, WS_Y2 + (f * D + wave * 128 + c16 * 8 + 2 * e4) * 8, __float_as_uint(a0), __float_as_uint(a1), tag);
      STAMP(9);
    }
    u32x4 pz[9];
    poll_y2(pz, y0, y1, rws, (r8 * D + (is_fin ? fin : 0) * 16 + c8 * 2) * 8, WS_Y2, tag, (unsigned)(wave - 5) >= 2u || f >= 20 ? 1 : 0);
    if (is_fin) {
      y0 += dpp<ROR8>(y0); y1 += dpp<ROR8>(y1);
      y0 = tr16(y0, y0); y1 = tr16(y1, y1); y0 = tr32(y0, y0); y1 = tr32(y1, y1);
      f32x2 of = xo + rbf2(f32x2{ga0, ga1} * rbf2(f32x2{y0, y1}));
      unsigned o = pack2(of[0], of[1]);
      __builtin_amdgcn_raw_buffer_store_b32(o, rout, lane < 8 ? (fin * 8 + lane) * 4 : DROP, 0, 0);
      __builtin_amdgcn_raw_buffer_store_b32(tag, rws, (fin == 0 && lane == 0) ? WS_EPOCH : DROP, 0, AUX_SC1);
      STAMP(9);
    }
    MEMBAR();
    keep_regs(pz);
  }
  STAMP_FLUSH();
}

extern "C" void kernel_launch(void* const* d_in, const int* in_sizes, int n_in, void* d_out, int out_size, void* d_ws,
                              size_t ws_size, hipStream_t stream) {
  fused_kernel<<<256, 512, 0, stream>>>((const u16*)d_in[0], (const u16*)d_in[1], (const u16*)d_in[2], (const u16*)d_in[3],
                                        (const u16*)d_in[4], (const u16*)d_in[6], (const u16*)d_in[7], (const u16*)d_in[8],
                                        (const u16*)d_in[9], (const u16*)d_in[10], (u16*)d_out, (unsigned char*)d_ws);
}
```
